```python
import torch

def _softmax(x, axis=-1):
    return torch.softmax(x, dim=axis)
def _gelu(x):
    return torch.nn.functional.gelu(x, approximate="tanh")
def _silu(x):
    return torch.nn.functional.silu(x)
def _sigmoid(x):
    return torch.sigmoid(x)
def _layernorm(x, axis=-1, eps=1e-5):
    m = x.mean(dim=axis, keepdim=True)
    v = x.var(dim=axis, keepdim=True, unbiased=False)
    return (x - m) / torch.sqrt(v + eps)
def _scatter_add(tgt, idx, src, axis=0):
    return tgt.clone().index_add_(axis, idx, src)
def _segment_sum(src, seg, n):
    out = torch.zeros((n,) + src.shape[1:], dtype=src.dtype)
    return out.index_add_(0, seg, src)
def _pad(x, pw):
    flat = tuple(v for pair in reversed(pw) for v in pair)
    return torch.nn.functional.pad(x, flat)

kernel_name = 'standalone__linear_kernel_rfa__decode'

def setup_inputs():

    _w = (lambda a: (lambda t: t.to(torch.bfloat16) if t.is_floating_point() else t)(torch.as_tensor(a)))
    B, H, Sq, Sk, D, M = 1, 32, 1, 4096, 128, 64
    return {
        "q": _w((lambda *s: torch.randn(*s, dtype=torch.bfloat16) * 0.2)(B, H, Sq, D)),
        "k": _w((lambda *s: torch.randn(*s, dtype=torch.bfloat16) * 1.6)(B, H, Sk, D)),
        "v": _w((lambda *s: torch.randn(*s, dtype=torch.bfloat16) * 1.6)(B, H, Sk, D)),
        "omega": _w((lambda *s: torch.randn(*s, dtype=torch.bfloat16) * 1.6)(M, D)),
    }

def reference(q, k, v, omega):
    d = q.shape[-1]
    m = omega.shape[0]
    scale = float(d) ** -0.25
    qs = q * scale
    ks = k * scale
    q_proj = torch.einsum("bhsd,md->bhsm", qs, omega)
    k_proj = torch.einsum("bhsd,md->bhsm", ks, omega)
    q_sq = 0.5 * torch.einsum("bhsd,bhsd->bhs", qs, qs)[..., None]
    k_sq = 0.5 * torch.einsum("bhsd,bhsd->bhs", ks, ks)[..., None]
    inv_sqrt_m = float(m) ** -0.5
    phi_q = torch.exp(q_proj - q_sq) * inv_sqrt_m + 1e-6
    phi_k = torch.exp(k_proj - k_sq) * inv_sqrt_m + 1e-6
    kv = torch.einsum("bhsm,bhsd->bhmd", phi_k, v)
    z  = torch.einsum("bhsm->bhm", phi_k)
    num = torch.einsum("bhqm,bhmd->bhqd", phi_q, kv)
    den = torch.einsum("bhqm,bhm->bhq", phi_q, z)[..., None] + 1e-6
    return num / den
```

```cpp
#include <hip/hip_runtime.h>
#include <hip/hip_bf16.h>
#include <stdint.h>

#define EMU 0

typedef unsigned u32x4 __attribute__((ext_vector_type(4)));
typedef float f32x4 __attribute__((ext_vector_type(4)));
typedef float f32x2 __attribute__((ext_vector_type(2)));
typedef short bf16x8 __attribute__((ext_vector_type(8)));
typedef __bf16 bf16x2 __attribute__((ext_vector_type(2)));
typedef __bf16 bf16x8b __attribute__((ext_vector_type(8)));

constexpr int H = 32, SK = 4096, D = 128, M = 64;
constexpr int NSPLIT = 8, KEYS_WG = SK / NSPLIT, NWAVE = 8, KEYS_W = KEYS_WG / NWAVE;
constexpr float SCALE = 0.29730177875068026f;
constexpr float LOG2E = 1.4426950408889634f;

__device__ __forceinline__ float lo_f(unsigned u) { return __uint_as_float(u << 16); }
__device__ __forceinline__ float hi_f(unsigned u) { return __uint_as_float(u & 0xffff0000u); }
__device__ __forceinline__ unsigned pk_bf16(float lo, float hi) {
  f32x2 v = {lo, hi};
  bf16x2 r = __builtin_convertvector(v, bf16x2);
  return __builtin_bit_cast(unsigned, r);
}
__device__ __forceinline__ float dot2(unsigned a, unsigned b, float acc) {
  return __builtin_amdgcn_fdot2_f32_bf16(__builtin_bit_cast(bf16x2, a), __builtin_bit_cast(bf16x2, b), acc, false);
}
__device__ __forceinline__ float xsum_rows(float x) {
  auto s = __builtin_amdgcn_permlane16_swap(__float_as_uint(x), __float_as_uint(x), false, false);
  x = __uint_as_float(s[0]) + __uint_as_float(s[1]);
  auto t = __builtin_amdgcn_permlane32_swap(__float_as_uint(x), __float_as_uint(x), false, false);
  return __uint_as_float(t[0]) + __uint_as_float(t[1]);
}

__device__ __forceinline__ void phi_tile(const u32x4 (&raw)[4], const u32x4 (&om)[4][4], float (&phi)[16]) {
  u32x4 ks[4];
  float sq = 0.f;
#pragma unroll
  for (int t = 0; t < 4; ++t) {
#pragma unroll
    for (int j = 0; j < 4; ++j) {
      ks[t][j] = raw[t][j];
      sq = dot2(ks[t][j], ks[t][j], sq);
    }
  }
  sq = xsum_rows(sq);
  sq = 0.5f * SCALE * SCALE * sq;
#pragma unroll
  for (int mt = 0; mt < 4; ++mt) {
    f32x4 acc = {0.f, 0.f, 0.f, 0.f};
#pragma unroll
    for (int t = 0; t < 4; ++t)
      acc = __builtin_amdgcn_mfma_f32_16x16x32_bf16(__builtin_bit_cast(bf16x8b, om[mt][t]), __builtin_bit_cast(bf16x8b, ks[t]), acc, 0, 0, 0);
#pragma unroll
    for (int p = 0; p < 2; ++p) {
      phi[mt * 4 + 2 * p] = fmaf(__builtin_amdgcn_exp2f((acc[2 * p] * SCALE - sq) * LOG2E), 0.125f, 1e-6f);
      phi[mt * 4 + 2 * p + 1] = fmaf(__builtin_amdgcn_exp2f((acc[2 * p + 1] * SCALE - sq) * LOG2E), 0.125f, 1e-6f);
    }
  }
}

__global__ void __launch_bounds__(512) rfa_part(const uint16_t* __restrict__ q, const uint16_t* __restrict__ k,
                                                const uint16_t* __restrict__ v, const uint16_t* __restrict__ omega,
                                                float* __restrict__ part) {
  __shared__ float red[NWAVE][132];
  const int bid = blockIdx.x, h = bid >> 3, sp = bid & 7;
  const int tid = threadIdx.x, lane = tid & 63, wave = __builtin_amdgcn_readfirstlane(tid >> 6);
  const int r = lane & 15, g = lane >> 4;
  const size_t key0 = (size_t)h * SK + sp * KEYS_WG + wave * KEYS_W;
  const uint16_t* kb = k + key0 * D;
  const uint16_t* vb = v + key0 * D;
  u32x4 qraw[4], om[4][4], kr[4][4], vr[16];
#pragma unroll
  for (int t = 0; t < 4; ++t) qraw[t] = *(const u32x4*)(q + h * D + 32 * t + 8 * g);
#pragma unroll
  for (int mt = 0; mt < 4; ++mt)
#pragma unroll
    for (int t = 0; t < 4; ++t) om[mt][t] = *(const u32x4*)(omega + (16 * mt + r) * D + 32 * t + 8 * g);
#pragma unroll
  for (int T = 0; T < 4; ++T)
#pragma unroll
    for (int t = 0; t < 4; ++t) kr[T][t] = *(const u32x4*)(kb + (16 * T + r) * D + 32 * t + 8 * g);
#pragma unroll
  for (int n = 0; n < 16; ++n) vr[n] = *(const u32x4*)(vb + (16 * g + n) * D + 8 * r);
  float pq[16];
  phi_tile(qraw, om, pq);
  float p[4];
#pragma unroll
  for (int T = 0; T < 4; ++T) {
    float pk[16];
    phi_tile(kr[T], om, pk);
    float s = 0.f;
#pragma unroll
    for (int i = 0; i < 16; ++i) s = fmaf(pq[i], pk[i], s);
    p[T] = s;
  }
  auto s02 = __builtin_amdgcn_permlane32_swap(__float_as_uint(p[0]), __float_as_uint(p[2]), false, false);
  float a = __uint_as_float(s02[0]) + __uint_as_float(s02[1]);
  auto s13 = __builtin_amdgcn_permlane32_swap(__float_as_uint(p[1]), __float_as_uint(p[3]), false, false);
  float b = __uint_as_float(s13[0]) + __uint_as_float(s13[1]);
  auto sab = __builtin_amdgcn_permlane16_swap(__float_as_uint(a), __float_as_uint(b), false, false);
  float w = __uint_as_float(sab[0]) + __uint_as_float(sab[1]);
  float acc[8];
#pragma unroll
  for (int i = 0; i < 8; ++i) acc[i] = 0.f;
#pragma unroll
  for (int n = 0; n < 16; ++n) {
    float wn = __shfl(w, (lane & 48) + n);
#pragma unroll
    for (int j = 0; j < 4; ++j) {
      acc[2 * j] = fmaf(wn, lo_f(vr[n][j]), acc[2 * j]);
      acc[2 * j + 1] = fmaf(wn, hi_f(vr[n][j]), acc[2 * j + 1]);
    }
  }
#pragma unroll
  for (int i = 0; i < 8; ++i) acc[i] = xsum_rows(acc[i]);
  float den = w;
#pragma unroll
  for (int o = 1; o < 64; o <<= 1) den += __shfl_xor(den, o);
  if (g == 0) {
#pragma unroll
    for (int i = 0; i < 8; ++i) red[wave][8 * r + i] = acc[i];
    if (r == 0) red[wave][128] = den;
  }
  __syncthreads();
  if (tid < 129) {
    float s = 0.f;
#pragma unroll
    for (int wv = 0; wv < NWAVE; ++wv) s += red[wv][tid];
    part[bid * 132 + tid] = s;
  }
}

__global__ void __launch_bounds__(128) rfa_comb(const float* __restrict__ part, uint16_t* __restrict__ out) {
  const int h = blockIdx.x, d = threadIdx.x;
  float num = 0.f, den = 0.f;
#pragma unroll
  for (int s = 0; s < NSPLIT; ++s) {
    num += part[(h * NSPLIT + s) * 132 + d];
    den += part[(h * NSPLIT + s) * 132 + 128];
  }
  float o = num / (den + 1e-6f);
  out[h * D + d] = (uint16_t)(pk_bf16(o, o) & 0xffffu);
}

extern "C" void kernel_launch(void* const* d_in, const int* in_sizes, int n_in, void* d_out, int out_size, void* d_ws,
                              size_t ws_size, hipStream_t stream) {
  const uint16_t* q = (const uint16_t*)d_in[0];
  const uint16_t* k = (const uint16_t*)d_in[1];
  const uint16_t* v = (const uint16_t*)d_in[2];
  const uint16_t* om = (const uint16_t*)d_in[3];
  rfa_part<<<H * NSPLIT, 512, 0, stream>>>(q, k, v, om, (float*)d_ws);
  rfa_comb<<<H, 128, 0, stream>>>((const float*)d_ws, (uint16_t*)d_out);
}
```

```cpp
#include <hip/hip_runtime.h>
#include <stdint.h>

#define EMU 0
#define NT 1
#define KPRE 0
#define INL 1
#define NTS " nt"

typedef unsigned u32x4 __attribute__((ext_vector_type(4)));
typedef unsigned u32x2 __attribute__((ext_vector_type(2)));
typedef float f32x4 __attribute__((ext_vector_type(4)));
typedef float f32x2 __attribute__((ext_vector_type(2)));
typedef __bf16 bf16x2 __attribute__((ext_vector_type(2)));
typedef __bf16 bf16x8 __attribute__((ext_vector_type(8)));
#define LAS __attribute__((address_space(3)))
#define GAS __attribute__((address_space(1)))
typedef GAS unsigned long long gu64;
typedef GAS unsigned gu32;

constexpr int H = 32, SK = 4096, D = 128;
constexpr int NSPLIT = 8, KEYS_WG = SK / NSPLIT, NWAVE = 8, KEYS_W = KEYS_WG / NWAVE;
constexpr float SCALE = 0.29730177875068026f;
constexpr float LOG2E = 1.4426950408889634f;
constexpr int OM_OFF = 0, Q_OFF = 16384, K_OFF = 16384 + 2048, RED_OFF = K_OFF + NWAVE * 16384, RED_STR = 132 * 4;
constexpr int LDS_BYTES = RED_OFF + NWAVE * RED_STR;
constexpr int WS_EPOCH = 0, WS_REC = 65536, REC_STR = 2048;

__device__ __forceinline__ float lo_f(unsigned u) { return __uint_as_float(u << 16); }
__device__ __forceinline__ float hi_f(unsigned u) { return __uint_as_float(u & 0xffff0000u); }
__device__ __forceinline__ unsigned pk_bf16(float lo, float hi) {
  f32x2 v = {lo, hi};
  return __builtin_bit_cast(unsigned, __builtin_convertvector(v, bf16x2));
}
__device__ __forceinline__ float dot2(unsigned a, unsigned b, float acc) {
  return __builtin_amdgcn_fdot2_f32_bf16(__builtin_bit_cast(bf16x2, a), __builtin_bit_cast(bf16x2, b), acc, false);
}
__device__ __forceinline__ float swap16_add(float a, float b) {
  auto s = __builtin_amdgcn_permlane16_swap(__float_as_uint(a), __float_as_uint(b), false, false);
  return __uint_as_float(s[0]) + __uint_as_float(s[1]);
}
__device__ __forceinline__ float swap32_add(float a, float b) {
  auto s = __builtin_amdgcn_permlane32_swap(__float_as_uint(a), __float_as_uint(b), false, false);
  return __uint_as_float(s[0]) + __uint_as_float(s[1]);
}
__device__ __forceinline__ float xsum_rows(float x) { x = swap16_add(x, x); return swap32_add(x, x); }
template <int CTRL> __device__ __forceinline__ float dpp(float x) {
  return __builtin_bit_cast(float, __builtin_amdgcn_mov_dpp(__builtin_bit_cast(int, x), CTRL, 0xf, 0xf, true));
}
#define STAMP(i)

template <int N> __device__ __forceinline__ void vload16(u32x4& dst, unsigned voff, const void* sbase) {
  asm volatile("global_load_dwordx4 %0, %1, %2 offset:%3" NTS : "=v"(dst) : "v"(voff), "s"(sbase), "i"(N * 256));
}
#define GLDS4(voff0, voff1, voff2, voff3, sbase, m0v)                                              \
  asm volatile("s_mov_b32 m0, %5\n\ts_nop 0\n\t"                                                    \
               "global_load_lds_dwordx4 %0, %4" NTS "\n\t"                                          \
               "global_load_lds_dwordx4 %1, %4 offset:1024" NTS "\n\t"                              \
               "global_load_lds_dwordx4 %2, %4 offset:2048" NTS "\n\t"                              \
               "global_load_lds_dwordx4 %3, %4 offset:3072" NTS                                     \
               :: "v"(voff0), "v"(voff1), "v"(voff2), "v"(voff3), "s"(sbase), "s"(m0v) : "memory")

template <int n> __device__ __forceinline__ void vstep(u32x4& vr, float w, f32x2 (&acc)[4]) {
  asm volatile("s_waitcnt vmcnt(%1)" : "+v"(vr) : "i"(15 - n));
  const float wn = dpp<0x150 + n>(w);
  const f32x2 w2 = {wn, wn};
#pragma unroll
  for (int j = 0; j < 4; ++j) {
    const f32x2 x = {lo_f(vr[j]), hi_f(vr[j])};
    acc[j] = __builtin_elementwise_fma(w2, x, acc[j]);
  }
}

__global__ void __launch_bounds__(512, 2) rfa_main(const uint16_t* q, const uint16_t* k, const uint16_t* v, const uint16_t* omega,
                                                   unsigned char* ws, uint16_t* out) {
  __shared__ __attribute__((aligned(16))) unsigned char lds[LDS_BYTES];
  const int bid = blockIdx.x, h = bid >> 3, sp = bid & 7;
  const int tid = threadIdx.x, lane = tid & 63, wave = __builtin_amdgcn_readfirstlane(tid >> 6);
  const int r = lane & 15, g = lane >> 4;
  const unsigned lbase = (unsigned)(uintptr_t)(LAS unsigned char*)lds;
  const size_t key0 = (size_t)h * SK + sp * KEYS_WG + wave * KEYS_W;
  const unsigned char* kb = (const unsigned char*)k + key0 * 256;
  const unsigned char* vb = (const unsigned char*)v + key0 * 256;
  const unsigned vo0 = (unsigned)((lane * 16) ^ (g * 16));
  gu64* rec = (gu64*)(ws + WS_REC + bid * REC_STR);
  unsigned ep = 0;
  if (wave == 0) {
    asm volatile("global_load_dword %0, %1, %2 sc1" : "=v"(ep) : "v"(0), "s"(ws + WS_EPOCH + h * 256));
    if (sp != 0) {
      __hip_atomic_store(rec + lane, 0ull, __ATOMIC_RELAXED, __HIP_MEMORY_SCOPE_AGENT);
      __hip_atomic_store(rec + 64 + lane, 0ull, __ATOMIC_RELAXED, __HIP_MEMORY_SCOPE_AGENT);
      if (lane == 0) __hip_atomic_store(rec + 128, 0ull, __ATOMIC_RELAXED, __HIP_MEMORY_SCOPE_AGENT);
    }
  }
  {
    const unsigned m0q = lbase + Q_OFF + wave * 256;
    asm volatile("s_mov_b32 m0, %2\n\ts_nop 0\n\tglobal_load_lds_dword %0, %1" ::"v"(lane * 4), "s"((const unsigned char*)q + h * 256), "s"(m0q) : "memory");
    const unsigned m0o = lbase + OM_OFF + wave * 2048;
    const unsigned x = (wave & 1) * 128;
    asm volatile("s_mov_b32 m0, %3\n\ts_nop 0\n\tglobal_load_lds_dwordx4 %0, %2\n\tglobal_load_lds_dwordx4 %1, %2 offset:1024"
                 ::"v"(vo0 ^ x), "v"(vo0 ^ x ^ 64), "s"((const unsigned char*)omega + wave * 2048), "s"(m0o) : "memory");
  }
  const unsigned kw = lbase + K_OFF + wave * 16384;
#pragma unroll
  for (int T = 0; T < KPRE; ++T) GLDS4(vo0, vo0 ^ 64, vo0 ^ 128, vo0 ^ 192, kb + T * 4096, kw + T * 4096);
  asm volatile("s_waitcnt vmcnt(%0)\n\ts_barrier" :: "i"(4 * KPRE) : "memory");
  const unsigned rd0 = r * 256 + ((g ^ r) * 16);
  u32x4 om[4][4];
#pragma unroll
  for (int mt = 0; mt < 4; ++mt)
#pragma unroll
    for (int t = 0; t < 4; ++t) om[mt][t] = *(const LAS u32x4*)(uintptr_t)(lbase + OM_OFF + mt * 4096 + (rd0 ^ (t * 64)));
#pragma unroll
  for (int T = KPRE; T < 4; ++T) GLDS4(vo0, vo0 ^ 64, vo0 ^ 128, vo0 ^ 192, kb + T * 4096, kw + T * 4096);
  u32x4 vr[16];
  const unsigned vv = g * 4096 + r * 16;
  vload16<0>(vr[0], vv, vb); vload16<1>(vr[1], vv, vb); vload16<2>(vr[2], vv, vb); vload16<3>(vr[3], vv, vb);
  vload16<4>(vr[4], vv, vb); vload16<5>(vr[5], vv, vb); vload16<6>(vr[6], vv, vb); vload16<7>(vr[7], vv, vb);
  vload16<8>(vr[8], vv, vb); vload16<9>(vr[9], vv, vb); vload16<10>(vr[10], vv, vb); vload16<11>(vr[11], vv, vb);
  vload16<12>(vr[12], vv, vb); vload16<13>(vr[13], vv, vb); vload16<14>(vr[14], vv, vb); vload16<15>(vr[15], vv, vb);
  float pq[16];
#pragma unroll
  for (int i = 0; i < 16; ++i) pq[i] = 0.f;
  float pqs = 0.f;
  float w = 0.f;
  unsigned a0 = lbase + Q_OFF + wave * 256 + g * 16, anext = kw + rd0;
#pragma nounroll
  for (int it = 0; it < 5; ++it) {
    if (it == 1) asm volatile("s_waitcnt vmcnt(28)" ::: "memory");
    else if (it == 2) asm volatile("s_waitcnt vmcnt(24)" ::: "memory");
    else if (it == 3) asm volatile("s_waitcnt vmcnt(20)" ::: "memory");
    else if (it == 4) asm volatile("s_waitcnt vmcnt(16)" ::: "memory");
    u32x4 ks[4];
#pragma unroll
    for (int t = 0; t < 4; ++t) ks[t] = *(const LAS u32x4*)(uintptr_t)(a0 ^ (t * 64));
    a0 = anext; anext += 4096;
    float sq = 0.f;
#pragma unroll
    for (int t = 0; t < 4; ++t)
#pragma unroll
      for (int j = 0; j < 4; ++j) {
        sq = dot2(ks[t][j], ks[t][j], sq);
      }
    sq = xsum_rows(sq);
    sq = (-0.5f * SCALE * SCALE * LOG2E) * sq;
    float s = 0.f;
#pragma unroll
    for (int mt = 0; mt < 4; ++mt) {
      f32x4 acc = {0.f, 0.f, 0.f, 0.f};
#pragma unroll
      for (int t = 0; t < 4; ++t)
        acc = __builtin_amdgcn_mfma_f32_16x16x32_bf16(__builtin_bit_cast(bf16x8, om[mt][t]), __builtin_bit_cast(bf16x8, ks[t]), acc, 0, 0, 0);
#pragma unroll
      for (int p = 0; p < 2; ++p) {
#pragma unroll
        for (int u = 0; u < 2; ++u) {
          const float e = __builtin_amdgcn_exp2f(fmaf(acc[2 * p + u], SCALE * LOG2E, sq));
          s = fmaf(e, pq[mt * 4 + 2 * p + u], s);
          if (it == 0) { const float ph = fmaf(e, 0.125f, 1e-6f); pq[mt * 4 + 2 * p + u] = 0.125f * ph; pqs += ph; }
        }
      }
    }
    s = fmaf(pqs, 1e-6f, s);
    s = xsum_rows(s);
    w = (g == it - 1) ? s : w;
  }
  float den = w;
  den += dpp<0xB1>(den); den += dpp<0x4E>(den); den += dpp<0x141>(den); den += dpp<0x128>(den);
  den = xsum_rows(den);
  f32x2 acc[4] = {{0.f, 0.f}, {0.f, 0.f}, {0.f, 0.f}, {0.f, 0.f}};
  vstep<0>(vr[0], w, acc); vstep<1>(vr[1], w, acc); vstep<2>(vr[2], w, acc); vstep<3>(vr[3], w, acc);
  vstep<4>(vr[4], w, acc); vstep<5>(vr[5], w, acc); vstep<6>(vr[6], w, acc); vstep<7>(vr[7], w, acc);
  vstep<8>(vr[8], w, acc); vstep<9>(vr[9], w, acc); vstep<10>(vr[10], w, acc); vstep<11>(vr[11], w, acc);
  vstep<12>(vr[12], w, acc); vstep<13>(vr[13], w, acc); vstep<14>(vr[14], w, acc); vstep<15>(vr[15], w, acc);
  {
    const float b0 = swap32_add(acc[0][0], acc[2][0]), b1 = swap32_add(acc[0][1], acc[2][1]);
    const float b2 = swap32_add(acc[1][0], acc[3][0]), b3 = swap32_add(acc[1][1], acc[3][1]);
    const f32x2 c = {swap16_add(b0, b2), swap16_add(b1, b3)};
    *(LAS f32x2*)(uintptr_t)(lbase + RED_OFF + wave * RED_STR + r * 32 + g * 8) = c;
    if (lane == 0) *(LAS float*)(uintptr_t)(lbase + RED_OFF + wave * RED_STR + 512) = den;
  }
  asm volatile("s_waitcnt lgkmcnt(0)\n\ts_barrier" ::: "memory");
  if (wave == 0) {
    f32x2 n2 = {0.f, 0.f};
    float dn = 0.f;
#pragma unroll
    for (int wv = 0; wv < NWAVE; ++wv) {
      n2 += *(const LAS f32x2*)(uintptr_t)(lbase + RED_OFF + wv * RED_STR + lane * 8);
      dn += *(const LAS float*)(uintptr_t)(lbase + RED_OFF + wv * RED_STR + 512);
    }
    asm volatile("" : "+v"(ep));
    const unsigned tag = (ep + 0x9E3779B8u) | 1u;
    if (sp != 0) {
      const unsigned long long t = (unsigned long long)tag << 32;
      __hip_atomic_store(rec + lane, t | __float_as_uint(n2[0]), __ATOMIC_RELAXED, __HIP_MEMORY_SCOPE_AGENT);
      __hip_atomic_store(rec + 64 + lane, t | __float_as_uint(n2[1]), __ATOMIC_RELAXED, __HIP_MEMORY_SCOPE_AGENT);
      if (lane == 0) __hip_atomic_store(rec + 128, t | __float_as_uint(dn), __ATOMIC_RELAXED, __HIP_MEMORY_SCOPE_AGENT);
    } else {
      unsigned long long x0[NSPLIT - 1], x1[NSPLIT - 1], xd;
      const int pl = (lane & 7) ? (lane & 7) : 1;
      for (int spin = 0; spin < (1 << 20); ++spin) {
        bool ok = true;
#pragma unroll
        for (int p = 1; p < NSPLIT; ++p) {
          x0[p - 1] = __hip_atomic_load(rec + p * (REC_STR / 8) + lane, __ATOMIC_RELAXED, __HIP_MEMORY_SCOPE_AGENT);
          x1[p - 1] = __hip_atomic_load(rec + p * (REC_STR / 8) + 64 + lane, __ATOMIC_RELAXED, __HIP_MEMORY_SCOPE_AGENT);
        }
        xd = __hip_atomic_load(rec + pl * (REC_STR / 8) + 128, __ATOMIC_RELAXED, __HIP_MEMORY_SCOPE_AGENT);
#pragma unroll
        for (int p = 1; p < NSPLIT; ++p) ok &= ((unsigned)(x0[p - 1] >> 32) == tag) & ((unsigned)(x1[p - 1] >> 32) == tag);
        ok &= (unsigned)(xd >> 32) == tag;
        if (__all(ok)) break;
      }
#pragma unroll
      for (int p = 1; p < NSPLIT; ++p) { n2[0] += __uint_as_float((unsigned)x0[p - 1]); n2[1] += __uint_as_float((unsigned)x1[p - 1]); }
      float dd = (lane & 7) ? __uint_as_float((unsigned)xd) : dn;
      dd += dpp<0xB1>(dd); dd += dpp<0x4E>(dd); dd += dpp<0x141>(dd);
      const float inv = 1.0f / (dd + 1e-6f);
      ((unsigned*)out)[h * 64 + lane] = pk_bf16(n2[0] * inv, n2[1] * inv);
      if (lane == 0) __hip_atomic_store((gu32*)(ws + WS_EPOCH + h * 256), tag, __ATOMIC_RELAXED, __HIP_MEMORY_SCOPE_AGENT);
    }
  }
}

extern "C" void kernel_launch(void* const* d_in, const int* in_sizes, int n_in, void* d_out, int out_size, void* d_ws,
                              size_t ws_size, hipStream_t stream) {
  const uint16_t* q = (const uint16_t*)d_in[0];
  const uint16_t* k = (const uint16_t*)d_in[1];
  const uint16_t* v = (const uint16_t*)d_in[2];
  const uint16_t* om = (const uint16_t*)d_in[3];
  rfa_main<<<H * NSPLIT, 512, 0, stream>>>(q, k, v, om, (unsigned char*)d_ws, (uint16_t*)d_out);
}
```

```cpp
#include <hip/hip_runtime.h>
#include <stdint.h>

#define NTS " nt"
#define SCHED 1
#define ROT 0
#define OUTSC1 0

typedef unsigned u32x4 __attribute__((ext_vector_type(4)));
typedef float f32x4 __attribute__((ext_vector_type(4)));
typedef float f32x2 __attribute__((ext_vector_type(2)));
typedef __bf16 bf16x2 __attribute__((ext_vector_type(2)));
typedef __bf16 bf16x8 __attribute__((ext_vector_type(8)));
#define LAS __attribute__((address_space(3)))
#define GAS __attribute__((address_space(1)))
typedef GAS unsigned long long gu64;
typedef GAS unsigned gu32;

constexpr int H = 32, SK = 4096, D = 128;
constexpr int NSPLIT = 8, NWAVE = 8;
constexpr float SCALE = 0.29730177875068026f;
constexpr float LOG2E = 1.4426950408889634f;
constexpr float C1 = SCALE * LOG2E;
constexpr float C2 = -0.5f * SCALE * SCALE * LOG2E;
constexpr int OM_OFF = 0, Q_OFF = 16384, K_OFF = 16384 + 2048, RED_OFF = K_OFF + NWAVE * 16384, RED_STR = 132 * 4;
constexpr int LDS_BYTES = RED_OFF + NWAVE * RED_STR;
constexpr int WS_EPOCH = 0, WS_REC = 65536, REC_STR = 2048;

__device__ __forceinline__ float lo_f(unsigned u) { return __uint_as_float(u << 16); }
__device__ __forceinline__ float hi_f(unsigned u) { return __uint_as_float(u & 0xffff0000u); }
__device__ __forceinline__ unsigned pk_bf16(float lo, float hi) {
  f32x2 v = {lo, hi};
  return __builtin_bit_cast(unsigned, __builtin_convertvector(v, bf16x2));
}
__device__ __forceinline__ float dot2(unsigned a, unsigned b, float acc) {
  return __builtin_amdgcn_fdot2_f32_bf16(__builtin_bit_cast(bf16x2, a), __builtin_bit_cast(bf16x2, b), acc, false);
}
__device__ __forceinline__ float swap16_add(float a, float b) {
  auto s = __builtin_amdgcn_permlane16_swap(__float_as_uint(a), __float_as_uint(b), false, false);
  return __uint_as_float(s[0]) + __uint_as_float(s[1]);
}
__device__ __forceinline__ float swap32_add(float a, float b) {
  auto s = __builtin_amdgcn_permlane32_swap(__float_as_uint(a), __float_as_uint(b), false, false);
  return __uint_as_float(s[0]) + __uint_as_float(s[1]);
}
__device__ __forceinline__ float xsum_rows(float x) { x = swap16_add(x, x); return swap32_add(x, x); }
template <int CTRL> __device__ __forceinline__ float dpp(float x) {
  return __builtin_bit_cast(float, __builtin_amdgcn_mov_dpp(__builtin_bit_cast(int, x), CTRL, 0xf, 0xf, true));
}
template <int CTRL, int ROWMASK> __device__ __forceinline__ float dpp_rows(float old, float x) {
  return __builtin_bit_cast(float, __builtin_amdgcn_update_dpp(__builtin_bit_cast(int, old), __builtin_bit_cast(int, x), CTRL, ROWMASK, 0xf, false));
}

#define GLDS4(voff0, voff1, voff2, voff3, sbase, m0v)                                              \
  asm volatile("s_mov_b32 m0, %5\n\ts_nop 0\n\t"                                                    \
               "global_load_lds_dwordx4 %0, %4" NTS "\n\t"                                          \
               "global_load_lds_dwordx4 %1, %4 offset:1024" NTS "\n\t"                              \
               "global_load_lds_dwordx4 %2, %4 offset:2048" NTS "\n\t"                              \
               "global_load_lds_dwordx4 %3, %4 offset:3072" NTS                                     \
               :: "v"(voff0), "v"(voff1), "v"(voff2), "v"(voff3), "s"(sbase), "s"(m0v) : "memory")
#define VLD4(d, voff, sbase)                                                                        \
  asm volatile("global_load_dwordx4 %0, %4, %5" NTS "\n\t"                                          \
               "global_load_dwordx4 %1, %4, %5 offset:1024" NTS "\n\t"                              \
               "global_load_dwordx4 %2, %4, %5 offset:2048" NTS "\n\t"                              \
               "global_load_dwordx4 %3, %4, %5 offset:3072" NTS                                     \
               : "=&v"(d[0]), "=&v"(d[1]), "=&v"(d[2]), "=&v"(d[3]) : "v"(voff), "s"(sbase) : "memory")
#define WAITV(n) asm volatile("s_waitcnt vmcnt(%0)" ::"i"(n) : "memory")

__device__ __forceinline__ void tile_exp(unsigned a0, const u32x4 (&om)[4][4], float (&e)[16]) {
  u32x4 ks[4];
#pragma unroll
  for (int t = 0; t < 4; ++t) ks[t] = *(const LAS u32x4*)(uintptr_t)(a0 ^ (t * 64));
  f32x4 acc[4];
  float sq[4];
#pragma unroll
  for (int t = 0; t < 4; ++t) {
#pragma unroll
    for (int mt = 0; mt < 4; ++mt)
      acc[mt] = __builtin_amdgcn_mfma_f32_16x16x32_bf16(__builtin_bit_cast(bf16x8, om[mt][t]), __builtin_bit_cast(bf16x8, ks[t]),
                                                        t == 0 ? f32x4{0.f, 0.f, 0.f, 0.f} : acc[mt], 0, 0, 0);
    sq[t] = dot2(ks[t][0], ks[t][0], 0.f);
#pragma unroll
    for (int j = 1; j < 4; ++j) sq[t] = dot2(ks[t][j], ks[t][j], sq[t]);
  }
  const float s = C2 * xsum_rows((sq[0] + sq[1]) + (sq[2] + sq[3]));
#pragma unroll
  for (int mt = 0; mt < 4; ++mt)
#pragma unroll
    for (int i = 0; i < 4; ++i) e[mt * 4 + i] = __builtin_amdgcn_exp2f(fmaf(acc[mt][i], C1, s));
}
__device__ __forceinline__ float tile_w(unsigned a0, const u32x4 (&om)[4][4], const float (&pq)[16], float pq0, float& den) {
  float e[16];
  tile_exp(a0, om, e);
  float s0 = pq0, s1 = 0.f, s2 = 0.f, s3 = 0.f;
#pragma unroll
  for (int i = 0; i < 4; ++i) {
    s0 = fmaf(e[i], pq[i], s0); s1 = fmaf(e[4 + i], pq[4 + i], s1);
    s2 = fmaf(e[8 + i], pq[8 + i], s2); s3 = fmaf(e[12 + i], pq[12 + i], s3);
  }
  float w = xsum_rows((s0 + s1) + (s2 + s3));
  den += w;
  float x = dpp_rows<0x12F, 2>(w, w);
  x = dpp_rows<0x12E, 4>(x, w);
  x = dpp_rows<0x12D, 8>(x, w);
  return x;
}
template <int J, int CNT> __device__ __forceinline__ void vstep(u32x4& vr, float w, f32x2 (&acc)[4]) {
  asm volatile("s_waitcnt vmcnt(%1)" : "+v"(vr) : "i"(CNT));
  const float wn = dpp<0x150 + 4 * J>(w);
  const f32x2 w2 = {wn, wn};
#pragma unroll
  for (int j = 0; j < 4; ++j) {
    const f32x2 x = {lo_f(vr[j]), hi_f(vr[j])};
    acc[j] = __builtin_elementwise_fma(w2, x, acc[j]);
  }
}
template <int CNT> __device__ __forceinline__ void vtile(u32x4 (&vr)[4], float w, f32x2 (&acc)[4]) {
  vstep<0, CNT + 3>(vr[0], w, acc); vstep<1, CNT + 2>(vr[1], w, acc); vstep<2, CNT + 1>(vr[2], w, acc); vstep<3, CNT>(vr[3], w, acc);
}
#define ST(i)

__global__ void __launch_bounds__(512, 2) rfa_main(const uint16_t* q, const uint16_t* k, const uint16_t* v, const uint16_t* omega,
                                                   unsigned char* ws, uint16_t* out) {
  __shared__ __attribute__((aligned(16))) unsigned char lds[LDS_BYTES];
  ST(0);
  const int bid = blockIdx.x;
  const int cw = (bid & 7) * 32 + (bid >> 3), h = cw >> 3, sp = cw & 7;
  const int tid = threadIdx.x, lane = tid & 63, wave = __builtin_amdgcn_readfirstlane(tid >> 6);
  const int r = lane & 15, g = lane >> 4;
  const unsigned lbase = (unsigned)(uintptr_t)(LAS unsigned char*)lds;
  const size_t off0 = (size_t)cw * (512 * 256) + wave * 4096;
  const unsigned char* kb = (const unsigned char*)k + off0;
  const unsigned char* vb = (const unsigned char*)v + off0;
  const unsigned vo0 = (unsigned)((lane * 16) ^ (g * 16));
  const unsigned kw = lbase + K_OFF + wave * 16384;
#define TOFF(T) (ROT ? (((T) + h) & 3) * 32768 : (T) * 32768)
#define KLD(T) GLDS4(vo0, vo0 ^ 64, vo0 ^ 128, vo0 ^ 192, kb + TOFF(T), kw + (T) * 4096)
#define VLD(T) VLD4(vr[T], lane * 16, vb + TOFF(T))
  {
    const unsigned m0q = lbase + Q_OFF + wave * 256;
    asm volatile("s_mov_b32 m0, %2\n\ts_nop 0\n\tglobal_load_lds_dword %0, %1" ::"v"(lane * 4), "s"((const unsigned char*)q + h * 256), "s"(m0q) : "memory");
    const unsigned m0o = lbase + OM_OFF + wave * 2048;
    const unsigned x = (wave & 1) * 128;
    asm volatile("s_mov_b32 m0, %3\n\ts_nop 0\n\tglobal_load_lds_dwordx4 %0, %2\n\tglobal_load_lds_dwordx4 %1, %2 offset:1024\n\ts_barrier"
                 ::"v"(vo0 ^ x), "v"(vo0 ^ x ^ 64), "s"((const unsigned char*)omega + wave * 2048), "s"(m0o) : "memory");
  }
  KLD(0);
  KLD(1);
  gu64* rec = (gu64*)(ws + WS_REC + cw * REC_STR);
  unsigned ep = 0;
  asm volatile("s_waitcnt vmcnt(8)\n\ts_barrier" ::: "memory");
  ST(1);
  if (wave == 0) {
    asm volatile("global_load_dword %0, %1, %2 sc1" : "=v"(ep) : "v"(0), "s"(ws + WS_EPOCH + h * 256));
    if (sp != 0) {
      __hip_atomic_store(rec + lane, 0ull, __ATOMIC_RELAXED, __HIP_MEMORY_SCOPE_AGENT);
      __hip_atomic_store(rec + 64 + lane, 0ull, __ATOMIC_RELAXED, __HIP_MEMORY_SCOPE_AGENT);
      if (lane == 0) __hip_atomic_store(rec + 128, 0ull, __ATOMIC_RELAXED, __HIP_MEMORY_SCOPE_AGENT);
    }
  }
  const unsigned rd0 = r * 256 + ((g ^ r) * 16);
  u32x4 om[4][4];
#pragma unroll
  for (int mt = 0; mt < 4; ++mt)
#pragma unroll
    for (int t = 0; t < 4; ++t) om[mt][t] = *(const LAS u32x4*)(uintptr_t)(lbase + OM_OFF + mt * 4096 + (rd0 ^ (t * 64)));
  float pq[16], pq0 = 0.f;
  tile_exp(lbase + Q_OFF + wave * 256 + g * 16, om, pq);
#pragma unroll
  for (int i = 0; i < 16; ++i) { const float ph = fmaf(pq[i], 0.125f, 1e-6f); pq0 += ph; pq[i] = 0.125f * ph; }
  pq0 *= 1e-6f;
  ST(2);
  u32x4 vr[4][4];
  float w[4], den = 0.f;
  const unsigned ka = kw + rd0;
  f32x2 acc[4] = {{0.f, 0.f}, {0.f, 0.f}, {0.f, 0.f}, {0.f, 0.f}};
  KLD(2); KLD(3); WAITV(12); w[0] = tile_w(ka, om, pq, pq0, den);
  VLD(0); WAITV(12); w[1] = tile_w(ka + 4096, om, pq, pq0, den);
  VLD(1); WAITV(12); w[2] = tile_w(ka + 8192, om, pq, pq0, den);
  VLD(2); WAITV(12); w[3] = tile_w(ka + 12288, om, pq, pq0, den);
  VLD(3);
  ST(3);
  vtile<12>(vr[0], w[0], acc); vtile<8>(vr[1], w[1], acc); vtile<4>(vr[2], w[2], acc); vtile<0>(vr[3], w[3], acc);
  ST(4);
  den += dpp<0xB1>(den); den += dpp<0x4E>(den); den += dpp<0x141>(den); den += dpp<0x128>(den);
  {
    const float b0 = swap32_add(acc[0][0], acc[2][0]), b1 = swap32_add(acc[0][1], acc[2][1]);
    const float b2 = swap32_add(acc[1][0], acc[3][0]), b3 = swap32_add(acc[1][1], acc[3][1]);
    const f32x2 c = {swap16_add(b0, b2), swap16_add(b1, b3)};
    *(LAS f32x2*)(uintptr_t)(lbase + RED_OFF + wave * RED_STR + r * 32 + g * 8) = c;
    if (lane == 0) *(LAS float*)(uintptr_t)(lbase + RED_OFF + wave * RED_STR + 512) = den;
  }
  asm volatile("s_waitcnt lgkmcnt(0)\n\ts_barrier" ::: "memory");
  ST(5);
  if (wave == 0) {
    f32x2 n2 = {0.f, 0.f};
    float dn = 0.f;
#pragma unroll
    for (int wv = 0; wv < NWAVE; ++wv) {
      n2 += *(const LAS f32x2*)(uintptr_t)(lbase + RED_OFF + wv * RED_STR + lane * 8);
      dn += *(const LAS float*)(uintptr_t)(lbase + RED_OFF + wv * RED_STR + 512);
    }
    asm volatile("" : "+v"(ep));
    const unsigned tag = (ep + 0x9E3779B8u) | 1u;
    if (sp != 0) {
      const unsigned long long t = (unsigned long long)tag << 32;
      __hip_atomic_store(rec + lane, t | __float_as_uint(n2[0]), __ATOMIC_RELAXED, __HIP_MEMORY_SCOPE_AGENT);
      __hip_atomic_store(rec + 64 + lane, t | __float_as_uint(n2[1]), __ATOMIC_RELAXED, __HIP_MEMORY_SCOPE_AGENT);
      if (lane == 0) __hip_atomic_store(rec + 128, t | __float_as_uint(dn), __ATOMIC_RELAXED, __HIP_MEMORY_SCOPE_AGENT);
    } else {
      unsigned long long x0[NSPLIT - 1], x1[NSPLIT - 1], xd;
      const int pl = (lane & 7) ? (lane & 7) : 1;
      for (int spin = 0; spin < (1 << 20); ++spin) {
        bool ok = true;
#pragma unroll
        for (int p = 1; p < NSPLIT; ++p) {
          x0[p - 1] = __hip_atomic_load(rec + p * (REC_STR / 8) + lane, __ATOMIC_RELAXED, __HIP_MEMORY_SCOPE_AGENT);
          x1[p - 1] = __hip_atomic_load(rec + p * (REC_STR / 8) + 64 + lane, __ATOMIC_RELAXED, __HIP_MEMORY_SCOPE_AGENT);
        }
        xd = __hip_atomic_load(rec + pl * (REC_STR / 8) + 128, __ATOMIC_RELAXED, __HIP_MEMORY_SCOPE_AGENT);
#pragma unroll
        for (int p = 1; p < NSPLIT; ++p) ok &= ((unsigned)(x0[p - 1] >> 32) == tag) & ((unsigned)(x1[p - 1] >> 32) == tag);
        ok &= (unsigned)(xd >> 32) == tag;
        if (__all(ok)) break;
      }
#pragma unroll
      for (int p = 1; p < NSPLIT; ++p) { n2[0] += __uint_as_float((unsigned)x0[p - 1]); n2[1] += __uint_as_float((unsigned)x1[p - 1]); }
      float dd = (lane & 7) ? __uint_as_float((unsigned)xd) : dn;
      dd += dpp<0xB1>(dd); dd += dpp<0x4E>(dd); dd += dpp<0x141>(dd);
      const float inv = 1.0f / (dd + 1e-6f);
      ((unsigned*)out)[h * 64 + lane] = pk_bf16(n2[0] * inv, n2[1] * inv);
      if (lane == 0) __hip_atomic_store((gu32*)(ws + WS_EPOCH + h * 256), tag, __ATOMIC_RELAXED, __HIP_MEMORY_SCOPE_AGENT);
    }
  }
}

extern "C" void kernel_launch(void* const* d_in, const int* in_sizes, int n_in, void* d_out, int out_size, void* d_ws,
                              size_t ws_size, hipStream_t stream) {
  const uint16_t* q = (const uint16_t*)d_in[0];
  const uint16_t* k = (const uint16_t*)d_in[1];
  const uint16_t* v = (const uint16_t*)d_in[2];
  const uint16_t* om = (const uint16_t*)d_in[3];
  rfa_main<<<H * NSPLIT, 512, 0, stream>>>(q, k, v, om, (unsigned char*)d_ws, (uint16_t*)d_out);
}
```

```cpp
#include <hip/hip_runtime.h>
#include <stdint.h>

typedef unsigned u32x4 __attribute__((ext_vector_type(4)));
typedef float f32x4 __attribute__((ext_vector_type(4)));
typedef float f32x2 __attribute__((ext_vector_type(2)));
typedef __bf16 bf16x2 __attribute__((ext_vector_type(2)));
typedef __bf16 bf16x8 __attribute__((ext_vector_type(8)));
#define LAS __attribute__((address_space(3)))

constexpr int H = 32, NSPLIT = 8, NWAVE = 8;
constexpr float SCALE = 0.29730177875068026f;
constexpr float LOG2E = 1.4426950408889634f;
constexpr float C1 = SCALE * LOG2E;
constexpr float C2 = -0.5f * SCALE * SCALE * LOG2E;
constexpr int RED_OFF = 0, DEN_OFF = 8192, OM_OFF = 8448, Q_OFF = OM_OFF + 16384, K_OFF = Q_OFF + 2048;
constexpr int LDS_BYTES = K_OFF + NWAVE * 16384;
constexpr int WS_EPOCH = 0, WS_REC = 65536, REC_STR = 1152;

__device__ __forceinline__ unsigned pk_bf16(float lo, float hi) {
  f32x2 v = {lo, hi};
  return __builtin_bit_cast(unsigned, __builtin_convertvector(v, bf16x2));
}
__device__ __forceinline__ float dot2(unsigned a, unsigned b, float acc) {
  return __builtin_amdgcn_fdot2_f32_bf16(__builtin_bit_cast(bf16x2, a), __builtin_bit_cast(bf16x2, b), acc, false);
}
__device__ __forceinline__ f32x4 mfma(const u32x4& a, const u32x4& b, const f32x4& c) {
  return __builtin_amdgcn_mfma_f32_16x16x32_bf16(__builtin_bit_cast(bf16x8, a), __builtin_bit_cast(bf16x8, b), c, 0, 0, 0);
}
__device__ __forceinline__ float xsum_rows(float x) {
  auto s = __builtin_amdgcn_permlane16_swap(__float_as_uint(x), __float_as_uint(x), false, false);
  x = __uint_as_float(s[0]) + __uint_as_float(s[1]);
  auto t = __builtin_amdgcn_permlane32_swap(__float_as_uint(x), __float_as_uint(x), false, false);
  return __uint_as_float(t[0]) + __uint_as_float(t[1]);
}
template <int CTRL> __device__ __forceinline__ float dpp(float x) {
  return __builtin_bit_cast(float, __builtin_amdgcn_mov_dpp(__builtin_bit_cast(int, x), CTRL, 0xf, 0xf, true));
}
template <int CTRL, int ROWMASK> __device__ __forceinline__ float dpp_rows(float old, float x) {
  return __builtin_bit_cast(float, __builtin_amdgcn_update_dpp(__builtin_bit_cast(int, old), __builtin_bit_cast(int, x), CTRL, ROWMASK, 0xf, false));
}

#define GLDS4(voff0, voff1, voff2, voff3, sbase, m0v)                                              \
  asm volatile("s_mov_b32 m0, %5\n\ts_nop 0\n\t"                                                    \
               "global_load_lds_dwordx4 %0, %4 nt\n\t"                                              \
               "global_load_lds_dwordx4 %1, %4 offset:1024 nt\n\t"                                  \
               "global_load_lds_dwordx4 %2, %4 offset:2048 nt\n\t"                                  \
               "global_load_lds_dwordx4 %3, %4 offset:3072 nt"                                      \
               :: "v"(voff0), "v"(voff1), "v"(voff2), "v"(voff3), "s"(sbase), "s"(m0v) : "memory")
#define VLD4(d, voff, sbase)                                                                        \
  asm volatile("global_load_dwordx4 %0, %4, %5 nt\n\t"                                              \
               "global_load_dwordx4 %1, %4, %5 offset:1024 nt\n\t"                                  \
               "global_load_dwordx4 %2, %4, %5 offset:2048 nt\n\t"                                  \
               "global_load_dwordx4 %3, %4, %5 offset:3072 nt"                                      \
               : "=&v"(d[0]), "=&v"(d[1]), "=&v"(d[2]), "=&v"(d[3]) : "v"(voff), "s"(sbase) : "memory")
#define WAITV(n) asm volatile("s_waitcnt vmcnt(%0)" ::"i"(n) : "memory")
#define PRIO(n) asm volatile("s_setprio " #n)
#define PAUSE(n) do { __builtin_amdgcn_sched_barrier(0); __builtin_amdgcn_s_sleep(n); __builtin_amdgcn_sched_barrier(0); } while (0)

__device__ __forceinline__ void tile_exp(unsigned a0, const u32x4 (&om)[4][4], float (&e)[16]) {
  u32x4 ks[4];
#pragma unroll
  for (int t = 0; t < 4; ++t) ks[t] = *(const LAS u32x4*)(uintptr_t)(a0 ^ (t * 64));
  f32x4 acc[4];
  float sq[4];
#pragma unroll
  for (int t = 0; t < 4; ++t) {
#pragma unroll
    for (int mt = 0; mt < 4; ++mt) acc[mt] = mfma(om[mt][t], ks[t], t == 0 ? f32x4{0.f, 0.f, 0.f, 0.f} : acc[mt]);
    sq[t] = dot2(ks[t][0], ks[t][0], 0.f);
#pragma unroll
    for (int j = 1; j < 4; ++j) sq[t] = dot2(ks[t][j], ks[t][j], sq[t]);
  }
  const float s = C2 * xsum_rows((sq[0] + sq[1]) + (sq[2] + sq[3]));
#pragma unroll
  for (int mt = 0; mt < 4; ++mt)
#pragma unroll
    for (int i = 0; i < 4; ++i) e[mt * 4 + i] = __builtin_amdgcn_exp2f(fmaf(acc[mt][i], C1, s));
}
__device__ __forceinline__ unsigned tile_w(unsigned a0, const u32x4 (&om)[4][4], const float (&pq)[16], float pq0, float& den) {
  float e[16];
  tile_exp(a0, om, e);
  float s0 = pq0, s1 = 0.f, s2 = 0.f, s3 = 0.f;
#pragma unroll
  for (int i = 0; i < 4; ++i) {
    s0 = fmaf(e[i], pq[i], s0); s1 = fmaf(e[4 + i], pq[4 + i], s1);
    s2 = fmaf(e[8 + i], pq[8 + i], s2); s3 = fmaf(e[12 + i], pq[12 + i], s3);
  }
  const float w = xsum_rows((s0 + s1) + (s2 + s3));
  den += w;
  float z = dpp_rows<0x00, 1>(w, w);
  z = dpp_rows<0x55, 2>(z, w); z = dpp_rows<0xAA, 4>(z, w); z = dpp_rows<0xFF, 8>(z, w);
  const unsigned hi = pk_bf16(z, z);
  const float zl = z - __uint_as_float(hi << 16);
  const unsigned lo = pk_bf16(zl, zl);
  return (threadIdx.x & 2) ? lo : hi;
}
template <int N> __device__ __forceinline__ unsigned ror_u(unsigned y) {
  if constexpr (N == 0) return y;
  else return (unsigned)__builtin_amdgcn_mov_dpp((int)y, 0x120 + N, 0xf, 0xf, true);
}
template <int J> __device__ __forceinline__ u32x4 a_op(unsigned y, const u32x4& msk) {
  return u32x4{ror_u<4 * ((0 - J) & 3)>(y) & msk[0], ror_u<4 * ((1 - J) & 3)>(y) & msk[1],
               ror_u<4 * ((2 - J) & 3)>(y) & msk[2], ror_u<4 * ((3 - J) & 3)>(y) & msk[3]};
}
template <int CNT> __device__ __forceinline__ void v_tile(u32x4 (&vr)[4], unsigned y, const u32x4& msk, f32x4& acc) {
  u32x4 a0 = a_op<0>(y, msk), a1 = a_op<1>(y, msk), a2 = a_op<2>(y, msk), a3 = a_op<3>(y, msk);
  asm volatile("s_waitcnt vmcnt(%5)" : "+v"(vr[0]), "+v"(a0), "+v"(a1), "+v"(a2), "+v"(a3) : "i"(CNT + 3));
  acc = mfma(a0, vr[0], acc);
  if (CNT == 0) __builtin_amdgcn_sched_barrier(0);
  asm volatile("s_waitcnt vmcnt(%1)" : "+v"(vr[1]) : "i"(CNT + 2));
  acc = mfma(a1, vr[1], acc);
  if (CNT == 0) __builtin_amdgcn_sched_barrier(0);
  asm volatile("s_waitcnt vmcnt(%1)" : "+v"(vr[2]) : "i"(CNT + 1));
  acc = mfma(a2, vr[2], acc);
  if (CNT == 0) __builtin_amdgcn_sched_barrier(0);
  asm volatile("s_waitcnt vmcnt(%1)" : "+v"(vr[3]) : "i"(CNT));
  acc = mfma(a3, vr[3], acc);
}

__global__ void __launch_bounds__(512, 2) rfa_decode(const uint16_t* q, const uint16_t* k, const uint16_t* v, const uint16_t* omega,
                                                     unsigned char* ws, uint16_t* out) {
  __shared__ __attribute__((aligned(256))) unsigned char lds[LDS_BYTES];
  const int bid = blockIdx.x;
  const int cw = (bid & 7) * 32 + (bid >> 3), h = cw >> 3, sp = cw & 7;
  const int lane = threadIdx.x & 63, wave = __builtin_amdgcn_readfirstlane(threadIdx.x >> 6);
  const int r = lane & 15, g = lane >> 4;
  const unsigned lbase = (unsigned)(uintptr_t)(LAS unsigned char*)lds;

  const unsigned off0 = (unsigned)cw * (512 * 256) + wave * 4096;
  const unsigned sw0 = (unsigned)((lane * 16) ^ (g * 16));
  unsigned vo0 = off0 + sw0, vo1 = vo0 ^ 64, vo2 = vo0 ^ 128, vo3 = vo0 ^ 192;
  unsigned oq = h * 256 + lane * 4, oo0 = wave * 2048 + (sw0 ^ ((wave & 1) * 128)), oo1 = oo0 ^ 64;
  unsigned kw = lbase + K_OFF + wave * 16384, mq = lbase + Q_OFF + wave * 256, mo = lbase + OM_OFF + wave * 2048, eoff = WS_EPOCH + h * 256;
  const int tsel = sp * 8 + 3 - wave;
  const unsigned tout = (unsigned)(tsel >> 31);
  unsigned ot = (tout & (h * 256 + (lane & 3) * 64)) | (~tout & (WS_REC + cw * REC_STR + wave * 128 + (lane & 1) * 64 + (lane & 2) * 512 * (wave == 0)));
  *(LAS unsigned*)(uintptr_t)(lbase + DEN_OFF + 64 + r * 4) = 0u;
  asm volatile("" : "+v"(vo0), "+v"(vo1), "+v"(vo2), "+v"(vo3), "+v"(oq), "+v"(oo0), "+v"(oo1), "+v"(ot), "+s"(kw), "+s"(mq), "+s"(mo), "+s"(eoff));
  __builtin_amdgcn_sched_barrier(0);
  const unsigned char* kb = (const unsigned char*)k;
  const unsigned char* vb = (const unsigned char*)v;
#define KLD(T) GLDS4(vo0, vo1, vo2, vo3, kb + (T) * 32768, kw + (T) * 4096)
#define VLD(T) VLD4(vr[T], off0 + lane * 16, vb + (T) * 32768)

  PRIO(3);
  KLD(0);
  unsigned tag;
  asm volatile("s_mov_b32 m0, %[mq]\n\ts_load_dword %[e], %[ws], %[eoff]\n\tglobal_load_lds_dword %[oq], %[q]\n\t"
               "s_mov_b32 m0, %[mo]\n\ts_nop 0\n\tglobal_load_lds_dwordx4 %[o0], %[om]\n\tglobal_load_lds_dwordx4 %[o1], %[om] offset:1024\n\t"
               "s_barrier"
               : [e] "=&s"(tag)
               : [mq] "s"(mq), [ws] "s"(ws), [eoff] "s"(eoff), [oq] "v"(oq), [q] "s"(q), [mo] "s"(mo), [o0] "v"(oo0), [o1] "v"(oo1), [om] "s"(omega)
               : "memory");
  KLD(1);
  unsigned touch;
  unsigned long long tb;
  asm volatile("s_cmp_lt_i32 %6, 0\n\ts_cselect_b64 %2, %5, %4\n\tglobal_load_dword %0, %3, %2 sc1\n\ts_waitcnt vmcnt(5) lgkmcnt(0)\n\ts_barrier"
               : "=&v"(touch), "+s"(tag), "=&s"(tb) : "v"(ot), "s"(ws), "s"(out), "s"(__builtin_amdgcn_readfirstlane(tsel)) : "memory", "scc");
  tag = __builtin_amdgcn_readfirstlane(tag) + 0x9E3779B9u;

  const unsigned rd0 = r * 256 + ((g ^ r) * 16);
  u32x4 om[4][4];
#pragma unroll
  for (int mt = 0; mt < 4; ++mt)
#pragma unroll
    for (int t = 0; t < 4; ++t) om[mt][t] = *(const LAS u32x4*)(uintptr_t)(lbase + OM_OFF + mt * 4096 + (rd0 ^ (t * 64)));
  float pq[16], pq0 = 0.f;
  tile_exp(lbase + Q_OFF + wave * 256 + g * 16, om, pq);
#pragma unroll
  for (int i = 0; i < 16; ++i) { const float ph = fmaf(pq[i], 0.125f, 1e-6f); pq0 += ph; pq[i] = 0.125f * ph; }
  pq0 *= 1e-6f;

  u32x4 vr[4][4];
  unsigned w[4];
  float den = 0.f;
  const unsigned ka = kw + rd0;
  KLD(2); KLD(3); PRIO(2); WAITV(12); PAUSE(12); w[0] = tile_w(ka, om, pq, pq0, den);
  VLD(0);                  WAITV(12); PAUSE(12); w[1] = tile_w(ka + 4096, om, pq, pq0, den);
  VLD(1); PRIO(1);         WAITV(12);            w[2] = tile_w(ka + 8192, om, pq, pq0, den);
  VLD(2); PRIO(0);         WAITV(12);            w[3] = tile_w(ka + 12288, om, pq, pq0, den);
  VLD(3);
  asm volatile("" ::"v"(touch));

  den += dpp<0xB1>(den); den += dpp<0x4E>(den); den += dpp<0x141>(den); den += dpp<0x128>(den);
  den += (sp | wave) ? 0.f : 1e-6f;
  const unsigned hm = (lane & 1) ? 0xffff0000u : 0x0000ffffu;
  const u32x4 msk = {(r >> 2) == 0 ? hm : 0u, (r >> 2) == 1 ? hm : 0u, (r >> 2) == 2 ? hm : 0u, (r >> 2) == 3 ? hm : 0u};
  unsigned ra = lbase + RED_OFF + lane * 8, da = lbase + DEN_OFF, wa = ra + wave * 512, wd = da + wave * 4;
  unsigned c_one = 1, c_l16 = lane * 16, c_d8 = (lane & 7) * 8, c_oo = r * 16 + g * 4;
  unsigned char* rec = ws + WS_REC + cw * REC_STR;
  unsigned char* po = (unsigned char*)out + h * 256;
  unsigned char* pe = ws + WS_EPOCH + h * 256;
  unsigned long long pm, fm;
  asm volatile("s_cmp_lg_u32 %[sp], 0\n\ts_cselect_b64 %[pm], -1, 0\n\ts_mov_b64 %[fm], 0"
               : [pm] "=s"(pm), [fm] "=s"(fm), "+v"(ra), "+v"(da), "+v"(wa), "+v"(wd), "+v"(c_one), "+v"(c_l16), "+v"(c_d8), "+v"(c_oo),
                 "+s"(rec), "+s"(po), "+s"(pe)
               : [sp] "s"(sp) : "scc");
  f32x4 pv = {0.f, 0.f, 0.f, 0.f};
  v_tile<12>(vr[0], w[0], msk, pv); v_tile<8>(vr[1], w[1], msk, pv); v_tile<4>(vr[2], w[2], msk, pv); v_tile<0>(vr[3], w[3], msk, pv);
  f32x2 n2 = {pv[0] + pv[2], pv[1] + pv[3]};

  {
    unsigned tk, t, cnt;
    unsigned long long m;
    asm volatile(
      "0:\n\t"
      "ds_write_b64 %[wa], v[96:97]\n\t"
      "s_mov_b64 exec, 1\n\t"
      "ds_write_b32 %[wd], v98\n\t"
      "ds_add_rtn_u32 v108, %[da], %[one] offset:64\n\t"
      "s_mov_b64 exec, -1\n\t"
      "ds_read_b128 v[110:113], %[da]\n\t"
      "ds_read_b128 v[114:117], %[da] offset:16\n\t"
      "ds_read2st64_b64 v[118:121], %[ra] offset1:1\n\t"
      "ds_read2st64_b64 v[122:125], %[ra] offset0:2 offset1:3\n\t"
      "ds_read2st64_b64 v[126:129], %[ra] offset0:4 offset1:5\n\t"
      "ds_read2st64_b64 v[130:133], %[ra] offset0:6 offset1:7\n\t"
      "v_mov_b32 v101, %[tg]\n\t"
      "v_mov_b32 v103, %[tg]\n\t"
      "v_mov_b32 v105, %[tg]\n\t"
      "s_waitcnt lgkmcnt(4)\n\t"
      "v_readfirstlane_b32 %[tk], v108\n\t"
      "v_pk_add_f32 v[110:111], v[110:111], v[114:115]\n\t"
      "v_pk_add_f32 v[112:113], v[112:113], v[116:117]\n\t"
      "s_cmp_eq_u32 %[tk], 7\n\t"
      "s_cselect_b64 %[m], -1, 0\n\t"
      "v_add_f32 v110, v110, v111\n\t"
      "v_add_f32 v112, v112, v113\n\t"
      "s_waitcnt lgkmcnt(2)\n\t"
      "v_pk_add_f32 v[118:119], v[118:119], v[120:121]\n\t"
      "v_add_f32 v104, v110, v112\n\t"
      "v_pk_add_f32 v[122:123], v[122:123], v[124:125]\n\t"
      "v_mul_f32 v109, 0x2f800000, v104\n\t"
      "s_waitcnt lgkmcnt(0)\n\t"
      "v_pk_add_f32 v[126:127], v[126:127], v[128:129]\n\t"
      "v_pk_add_f32 v[130:131], v[130:131], v[132:133]\n\t"
      "v_pk_add_f32 v[118:119], v[118:119], v[122:123]\n\t"
      "v_rcp_f32 v109, v109\n\t"
      "v_pk_add_f32 v[126:127], v[126:127], v[130:131]\n\t"
      "s_nop 0\n\t"
      "v_add_f32 v100, v118, v126\n\t"
      "v_add_f32 v102, v119, v127\n\t"
      "s_and_b64 exec, %[m], %[pm]\n\t"
      "global_store_dwordx4 %[l16], v[100:103], %[rec] sc1\n\t"
      "s_and_b64 exec, exec, 0xff\n\t"
      "global_store_dwordx2 %[d8], v[104:105], %[rec] offset:1024 sc1\n\t"
      "s_and_b64 exec, %[m], %[fm]\n\t"
      "v_mul_f32 v110, v100, v109\n\t"
      "v_mul_f32 v111, v102, v109\n\t"
      "v_mul_f32 v110, 0x2f800000, v110\n\t"
      "v_mul_f32 v111, 0x2f800000, v111\n\t"
      "v_cvt_pk_bf16_f32 v110, v110, v111\n\t"
      "global_store_dword %[oo], v110, %[out] sc1\n\t"
      "s_and_b64 exec, exec, 1\n\t"
      "global_store_dword %[oo], v101, %[ep] sc1\n\t"
      "s_mov_b64 exec, -1\n\t"
      "s_or_b64 %[fm], %[fm], %[pm]\n\t"
      "s_cbranch_scc1 9f\n\t"
      "s_mov_b64 %[fm], -1\n\t"
      "v_add_u32 %[ra], 0x1000, %[ra]\n\t"
      "v_add_u32 %[da], 32, %[da]\n\t"
      "v_mov_b32 v96, v100\n\t"
      "v_mov_b32 v97, v102\n\t"
      "v_mov_b32 v98, v104\n\t"
      "s_add_u32 %[tk], %[tk], 1\n\t"
      "s_and_b32 %[tk], %[tk], 7\n\t"
      "v_lshl_add_u32 %[wa], %[tk], 9, %[ra]\n\t"
      "v_lshl_add_u32 %[wd], %[tk], 2, %[da]\n\t"
      "s_cbranch_scc0 0b\n\t"
      "s_mul_i32 %[t], %[tk], %[str]\n\t"
      "s_mov_b32 %[cnt], 0x100000\n\t"
      "v_add_u32 v106, %[t], %[l16]\n\t"
      "v_add_u32 v107, %[t], %[d8]\n\t"
      "1:\n\t"
      "global_load_dwordx4 v[100:103], v106, %[rec] sc1\n\t"
      "global_load_dwordx2 v[98:99], v107, %[rec] offset:1024 sc1\n\t"
      "s_waitcnt vmcnt(0)\n\t"
      "v_cmp_ne_u32 vcc, %[tg], v101\n\t"
      "v_cmp_ne_u32 %[m], %[tg], v103\n\t"
      "v_mov_b32 v96, v100\n\t"
      "s_or_b64 vcc, vcc, %[m]\n\t"
      "v_cmp_ne_u32 %[m], %[tg], v99\n\t"
      "v_mov_b32 v97, v102\n\t"
      "s_or_b64 vcc, vcc, %[m]\n\t"
      "s_cbranch_scc0 0b\n\t"
      "s_sub_u32 %[cnt], %[cnt], 1\n\t"
      "s_cbranch_scc0 1b\n\t"
      "s_branch 0b\n\t"
      "9:"
      : [tk] "=&s"(tk), [m] "=&s"(m), [t] "=&s"(t), [cnt] "=&s"(cnt), [fm] "+s"(fm), [ra] "+v"(ra), [da] "+v"(da), [wa] "+v"(wa), [wd] "+v"(wd),
        "+{v[96:97]}"(n2), "+{v98}"(den)
      : [pm] "s"(pm), [one] "v"(c_one), [l16] "v"(c_l16), [d8] "v"(c_d8), [rec] "s"(rec), [oo] "v"(c_oo), [out] "s"(po), [ep] "s"(pe),
        [tg] "s"(tag), [str] "i"(REC_STR)
      : "memory", "vcc", "scc", "v99", "v100", "v101", "v102", "v103", "v104", "v105", "v106", "v107", "v108", "v109", "v110", "v111", "v112",
        "v113", "v114", "v115", "v116", "v117", "v118", "v119", "v120", "v121", "v122", "v123", "v124", "v125", "v126", "v127", "v128",
        "v129", "v130", "v131", "v132", "v133");
  }
}

extern "C" void kernel_launch(void* const* d_in, const int* in_sizes, int n_in, void* d_out, int out_size, void* d_ws,
                              size_t ws_size, hipStream_t stream) {
  rfa_decode<<<H * NSPLIT, NWAVE * 64, 0, stream>>>((const uint16_t*)d_in[0], (const uint16_t*)d_in[1], (const uint16_t*)d_in[2],
                                                    (const uint16_t*)d_in[3], (unsigned char*)d_ws, (uint16_t*)d_out);
}
```

```cpp
#include <hip/hip_runtime.h>
#include <stdint.h>

typedef unsigned u32x4 __attribute__((ext_vector_type(4)));
typedef float f32x4 __attribute__((ext_vector_type(4)));
typedef float f32x2 __attribute__((ext_vector_type(2)));
typedef __bf16 bf16x2 __attribute__((ext_vector_type(2)));
typedef __bf16 bf16x8 __attribute__((ext_vector_type(8)));
#define LAS __attribute__((address_space(3)))

constexpr int H = 32, NSPLIT = 8, NWAVE = 8;
constexpr float SCALE = 0.29730177875068026f;
constexpr float LOG2E = 1.4426950408889634f;
constexpr float C1 = SCALE * LOG2E;
constexpr float C2 = -0.5f * SCALE * SCALE * LOG2E;
constexpr int RED_OFF = 0, DEN_OFF = 8192, OM_OFF = 8448, Q_OFF = OM_OFF + 16384, K_OFF = Q_OFF + 2048;
constexpr int LDS_BYTES = K_OFF + NWAVE * 16384;
constexpr int WS_EPOCH = 0, WS_REC = 65536, REC_STR = 1152;

__device__ __forceinline__ unsigned pk_bf16(float lo, float hi) {
  f32x2 v = {lo, hi};
  return __builtin_bit_cast(unsigned, __builtin_convertvector(v, bf16x2));
}
__device__ __forceinline__ float dot2(unsigned a, unsigned b, float acc) {
  return __builtin_amdgcn_fdot2_f32_bf16(__builtin_bit_cast(bf16x2, a), __builtin_bit_cast(bf16x2, b), acc, false);
}
__device__ __forceinline__ f32x4 mfma(const u32x4& a, const u32x4& b, const f32x4& c) {
  return __builtin_amdgcn_mfma_f32_16x16x32_bf16(__builtin_bit_cast(bf16x8, a), __builtin_bit_cast(bf16x8, b), c, 0, 0, 0);
}
__device__ __forceinline__ float xsum_rows(float x) {
  auto s = __builtin_amdgcn_permlane16_swap(__float_as_uint(x), __float_as_uint(x), false, false);
  x = __uint_as_float(s[0]) + __uint_as_float(s[1]);
  auto t = __builtin_amdgcn_permlane32_swap(__float_as_uint(x), __float_as_uint(x), false, false);
  return __uint_as_float(t[0]) + __uint_as_float(t[1]);
}
template <int CTRL> __device__ __forceinline__ float dpp(float x) {
  return __builtin_bit_cast(float, __builtin_amdgcn_mov_dpp(__builtin_bit_cast(int, x), CTRL, 0xf, 0xf, true));
}
template <int CTRL, int ROWMASK> __device__ __forceinline__ float dpp_rows(float old, float x) {
  return __builtin_bit_cast(float, __builtin_amdgcn_update_dpp(__builtin_bit_cast(int, old), __builtin_bit_cast(int, x), CTRL, ROWMASK, 0xf, false));
}

#define GLDS4(voff0, voff1, voff2, voff3, sbase, m0v)                                              \
  asm volatile("s_mov_b32 m0, %5\n\ts_nop 0\n\t"                                                    \
               "global_load_lds_dwordx4 %0, %4 nt\n\t"                                              \
               "global_load_lds_dwordx4 %1, %4 offset:1024 nt\n\t"                                  \
               "global_load_lds_dwordx4 %2, %4 offset:2048 nt\n\t"                                  \
               "global_load_lds_dwordx4 %3, %4 offset:3072 nt"                                      \
               :: "v"(voff0), "v"(voff1), "v"(voff2), "v"(voff3), "s"(sbase), "s"(m0v) : "memory")
#define VLD4(d, voff, sbase)                                                                        \
  asm volatile("global_load_dwordx4 %0, %4, %5 nt\n\t"                                              \
               "global_load_dwordx4 %1, %4, %5 offset:1024 nt\n\t"                                  \
               "global_load_dwordx4 %2, %4, %5 offset:2048 nt\n\t"                                  \
               "global_load_dwordx4 %3, %4, %5 offset:3072 nt"                                      \
               : "=&v"(d[0]), "=&v"(d[1]), "=&v"(d[2]), "=&v"(d[3]) : "v"(voff), "s"(sbase) : "memory")
#define WAITV(n) asm volatile("s_waitcnt vmcnt(%0)" ::"i"(n) : "memory")
#define PRIO(n) asm volatile("s_setprio " #n)
#define PAUSE(n) do { __builtin_amdgcn_sched_barrier(0); __builtin_amdgcn_s_sleep(n); __builtin_amdgcn_sched_barrier(0); } while (0)

__device__ __forceinline__ void tile_exp(unsigned a0, const u32x4 (&om)[4][4], float (&e)[16]) {
  u32x4 ks[4];
#pragma unroll
  for (int t = 0; t < 4; ++t) ks[t] = *(const LAS u32x4*)(uintptr_t)(a0 ^ (t * 64));
  f32x4 acc[4];
  float sq[4];
#pragma unroll
  for (int t = 0; t < 4; ++t) {
#pragma unroll
    for (int mt = 0; mt < 4; ++mt) acc[mt] = mfma(om[mt][t], ks[t], t == 0 ? f32x4{0.f, 0.f, 0.f, 0.f} : acc[mt]);
    sq[t] = dot2(ks[t][0], ks[t][0], 0.f);
#pragma unroll
    for (int j = 1; j < 4; ++j) sq[t] = dot2(ks[t][j], ks[t][j], sq[t]);
  }
  const float s = C2 * xsum_rows((sq[0] + sq[1]) + (sq[2] + sq[3]));
#pragma unroll
  for (int mt = 0; mt < 4; ++mt)
#pragma unroll
    for (int i = 0; i < 4; ++i) e[mt * 4 + i] = __builtin_amdgcn_exp2f(fmaf(acc[mt][i], C1, s));
}
__device__ __forceinline__ unsigned tile_w(unsigned a0, const u32x4 (&om)[4][4], const float (&pq)[16], float pq0, float& den) {
  float e[16];
  tile_exp(a0, om, e);
  float s0 = pq0, s1 = 0.f, s2 = 0.f, s3 = 0.f;
#pragma unroll
  for (int i = 0; i < 4; ++i) {
    s0 = fmaf(e[i], pq[i], s0); s1 = fmaf(e[4 + i], pq[4 + i], s1);
    s2 = fmaf(e[8 + i], pq[8 + i], s2); s3 = fmaf(e[12 + i], pq[12 + i], s3);
  }
  const float w = xsum_rows((s0 + s1) + (s2 + s3));
  den += w;
  float z = dpp_rows<0x00, 1>(w, w);
  z = dpp_rows<0x55, 2>(z, w); z = dpp_rows<0xAA, 4>(z, w); z = dpp_rows<0xFF, 8>(z, w);
  const unsigned hi = pk_bf16(z, z);
  const float zl = z - __uint_as_float(hi << 16);
  const unsigned lo = pk_bf16(zl, zl);
  return (threadIdx.x & 2) ? lo : hi;
}
template <int N> __device__ __forceinline__ unsigned ror_u(unsigned y) {
  if constexpr (N == 0) return y;
  else return (unsigned)__builtin_amdgcn_mov_dpp((int)y, 0x120 + N, 0xf, 0xf, true);
}
template <int J> __device__ __forceinline__ u32x4 a_op(unsigned y, const u32x4& msk) {
  return u32x4{ror_u<4 * ((0 - J) & 3)>(y) & msk[0], ror_u<4 * ((1 - J) & 3)>(y) & msk[1],
               ror_u<4 * ((2 - J) & 3)>(y) & msk[2], ror_u<4 * ((3 - J) & 3)>(y) & msk[3]};
}
template <int CNT> __device__ __forceinline__ void v_tile(u32x4 (&vr)[4], unsigned y, const u32x4& msk, f32x4& acc) {
  u32x4 a0 = a_op<0>(y, msk), a1 = a_op<1>(y, msk), a2 = a_op<2>(y, msk), a3 = a_op<3>(y, msk);
  asm volatile("s_waitcnt vmcnt(%5)" : "+v"(vr[0]), "+v"(a0), "+v"(a1), "+v"(a2), "+v"(a3) : "i"(CNT + 3));
  acc = mfma(a0, vr[0], acc);
  if (CNT == 0) __builtin_amdgcn_sched_barrier(0);
  asm volatile("s_waitcnt vmcnt(%1)" : "+v"(vr[1]) : "i"(CNT + 2));
  acc = mfma(a1, vr[1], acc);
  if (CNT == 0) __builtin_amdgcn_sched_barrier(0);
  asm volatile("s_waitcnt vmcnt(%1)" : "+v"(vr[2]) : "i"(CNT + 1));
  acc = mfma(a2, vr[2], acc);
  if (CNT == 0) __builtin_amdgcn_sched_barrier(0);
  asm volatile("s_waitcnt vmcnt(%1)" : "+v"(vr[3]) : "i"(CNT));
  acc = mfma(a3, vr[3], acc);
}

__global__ void __launch_bounds__(512, 2) rfa_decode(const uint16_t* q, const uint16_t* k, const uint16_t* v, const uint16_t* omega,
                                                     unsigned char* ws, uint16_t* out) {
  __shared__ __attribute__((aligned(256))) unsigned char lds[LDS_BYTES];
  const int bid = blockIdx.x;
  const int cw = (bid & 7) * 32 + (bid >> 3), h = cw >> 3, sp = cw & 7;
  const int lane = threadIdx.x & 63, wave = __builtin_amdgcn_readfirstlane(threadIdx.x >> 6);
  const int r = lane & 15, g = lane >> 4;
  const unsigned lbase = (unsigned)(uintptr_t)(LAS unsigned char*)lds;

  const unsigned off0 = (unsigned)cw * (512 * 256) + wave * 4096;
  const unsigned sw0 = (unsigned)((lane * 16) ^ (g * 16));
  unsigned vo0 = off0 + sw0, vo1 = vo0 ^ 64, vo2 = vo0 ^ 128, vo3 = vo0 ^ 192;
  unsigned oq = h * 256 + lane * 4, oo0 = wave * 2048 + (sw0 ^ ((wave & 1) * 128)), oo1 = oo0 ^ 64;
  unsigned kw = lbase + K_OFF + wave * 16384, mq = lbase + Q_OFF + wave * 256, mo = lbase + OM_OFF + wave * 2048, eoff = WS_EPOCH + h * 256;
  unsigned poff = ((bid >> 3) * 8 + wave) * 64;
  const int tsel = sp * 8 + 3 - wave;
  const unsigned tout = (unsigned)(tsel >> 31);
  unsigned ot = (tout & (h * 256 + (lane & 3) * 64)) | (~tout & (WS_REC + cw * REC_STR + wave * 128 + (lane & 1) * 64 + (lane & 2) * 512 * (wave == 0)));
  *(LAS unsigned*)(uintptr_t)(lbase + DEN_OFF + 64 + r * 4) = 0u;
  asm volatile("" : "+v"(vo0), "+v"(vo1), "+v"(vo2), "+v"(vo3), "+v"(oq), "+v"(oo0), "+v"(oo1), "+v"(ot), "+s"(kw), "+s"(mq), "+s"(mo), "+s"(eoff), "+s"(poff));
  __builtin_amdgcn_sched_barrier(0);
  const unsigned char* kb = (const unsigned char*)k;
  const unsigned char* vb = (const unsigned char*)v;
#define KLD(T) GLDS4(vo0, vo1, vo2, vo3, kb + (T) * 32768, kw + (T) * 4096)
#define VLD(T) VLD4(vr[T], off0 + lane * 16, vb + (T) * 32768)

  PRIO(3);
  KLD(0);
  unsigned tag, prime;
  asm volatile("s_load_dword %[pr], %[om], %[poff]\n\t"
               "s_mov_b32 m0, %[mq]\n\ts_load_dword %[e], %[ws], %[eoff]\n\tglobal_load_lds_dword %[oq], %[q]\n\t"
               "s_mov_b32 m0, %[mo]\n\ts_nop 0\n\tglobal_load_lds_dwordx4 %[o0], %[om]\n\tglobal_load_lds_dwordx4 %[o1], %[om] offset:1024\n\t"
               "s_barrier"
               : [e] "=&s"(tag), [pr] "=&s"(prime)
               : [mq] "s"(mq), [ws] "s"(ws), [eoff] "s"(eoff), [oq] "v"(oq), [q] "s"(q), [mo] "s"(mo), [o0] "v"(oo0), [o1] "v"(oo1), [om] "s"(omega),
                 [poff] "s"(poff)
               : "memory");
  KLD(1);
  unsigned touch;
  unsigned long long tb;
  asm volatile("s_cmp_lt_i32 %6, 0\n\ts_cselect_b64 %2, %5, %4\n\tglobal_load_dword %0, %3, %2 sc1\n\ts_waitcnt vmcnt(5) lgkmcnt(0)\n\ts_barrier"
               : "=&v"(touch), "+s"(tag), "=&s"(tb) : "v"(ot), "s"(ws), "s"(out), "s"(__builtin_amdgcn_readfirstlane(tsel)), "s"(prime)
               : "memory", "scc");
  tag = __builtin_amdgcn_readfirstlane(tag) + 0x9E3779B9u;

  const unsigned rd0 = r * 256 + ((g ^ r) * 16);
  u32x4 om[4][4];
#pragma unroll
  for (int mt = 0; mt < 4; ++mt)
#pragma unroll
    for (int t = 0; t < 4; ++t) om[mt][t] = *(const LAS u32x4*)(uintptr_t)(lbase + OM_OFF + mt * 4096 + (rd0 ^ (t * 64)));
  float pq[16], pq0 = 0.f;
  tile_exp(lbase + Q_OFF + wave * 256 + g * 16, om, pq);
#pragma unroll
  for (int i = 0; i < 16; ++i) { const float ph = fmaf(pq[i], 0.125f, 1e-6f); pq0 += ph; pq[i] = 0.125f * ph; }
  pq0 *= 1e-6f;

  u32x4 vr[4][4];
  unsigned w[4];
  float den = 0.f;
  const unsigned ka = kw + rd0;
  KLD(2); KLD(3); PRIO(2); WAITV(12); PAUSE(12); w[0] = tile_w(ka, om, pq, pq0, den);
  VLD(0);                  WAITV(12); PAUSE(12); w[1] = tile_w(ka + 4096, om, pq, pq0, den);
  VLD(1); PRIO(1);         WAITV(12);            w[2] = tile_w(ka + 8192, om, pq, pq0, den);
  VLD(2); PRIO(0);         WAITV(12);            w[3] = tile_w(ka + 12288, om, pq, pq0, den);
  VLD(3);
  asm volatile("" ::"v"(touch));

  den += dpp<0xB1>(den); den += dpp<0x4E>(den); den += dpp<0x141>(den); den += dpp<0x128>(den);
  den += (sp | wave) ? 0.f : 1e-6f;
  const unsigned hm = (lane & 1) ? 0xffff0000u : 0x0000ffffu;
  const u32x4 msk = {(r >> 2) == 0 ? hm : 0u, (r >> 2) == 1 ? hm : 0u, (r >> 2) == 2 ? hm : 0u, (r >> 2) == 3 ? hm : 0u};
  unsigned ra = lbase + RED_OFF + lane * 8, da = lbase + DEN_OFF, wa = ra + wave * 512, wd = da + wave * 4;
  unsigned c_one = 1, c_l16 = lane * 16, c_d8 = (lane & 7) * 8, c_oo = r * 16 + g * 4;
  unsigned char* rec = ws + WS_REC + cw * REC_STR;
  unsigned char* po = (unsigned char*)out + h * 256;
  unsigned char* pe = ws + WS_EPOCH + h * 256;
  unsigned long long pm, fm;
  asm volatile("s_cmp_lg_u32 %[sp], 0\n\ts_cselect_b64 %[pm], -1, 0\n\ts_mov_b64 %[fm], 0"
               : [pm] "=s"(pm), [fm] "=s"(fm), "+v"(ra), "+v"(da), "+v"(wa), "+v"(wd), "+v"(c_one), "+v"(c_l16), "+v"(c_d8), "+v"(c_oo),
                 "+s"(rec), "+s"(po), "+s"(pe)
               : [sp] "s"(sp) : "scc");
  f32x4 pv = {0.f, 0.f, 0.f, 0.f};
  v_tile<12>(vr[0], w[0], msk, pv); v_tile<8>(vr[1], w[1], msk, pv); v_tile<4>(vr[2], w[2], msk, pv); v_tile<0>(vr[3], w[3], msk, pv);
  f32x2 n2 = {pv[0] + pv[2], pv[1] + pv[3]};

  {
    unsigned tk, t, cnt;
    unsigned long long m;
    asm volatile(
      "0:\n\t"
      "ds_write_b64 %[wa], v[96:97]\n\t"
      "s_mov_b64 exec, 1\n\t"
      "ds_write_b32 %[wd], v98\n\t"
      "ds_add_rtn_u32 v108, %[da], %[one] offset:64\n\t"
      "s_mov_b64 exec, -1\n\t"
      "ds_read_b128 v[110:113], %[da]\n\t"
      "ds_read_b128 v[114:117], %[da] offset:16\n\t"
      "ds_read2st64_b64 v[118:121], %[ra] offset1:1\n\t"
      "ds_read2st64_b64 v[122:125], %[ra] offset0:2 offset1:3\n\t"
      "ds_read2st64_b64 v[126:129], %[ra] offset0:4 offset1:5\n\t"
      "ds_read2st64_b64 v[130:133], %[ra] offset0:6 offset1:7\n\t"
      "v_mov_b32 v101, %[tg]\n\t"
      "v_mov_b32 v103, %[tg]\n\t"
      "v_mov_b32 v105, %[tg]\n\t"
      "s_waitcnt lgkmcnt(4)\n\t"
      "v_readfirstlane_b32 %[tk], v108\n\t"
      "v_pk_add_f32 v[110:111], v[110:111], v[114:115]\n\t"
      "v_pk_add_f32 v[112:113], v[112:113], v[116:117]\n\t"
      "s_cmp_eq_u32 %[tk], 7\n\t"
      "s_cselect_b64 %[m], -1, 0\n\t"
      "v_add_f32 v110, v110, v111\n\t"
      "v_add_f32 v112, v112, v113\n\t"
      "s_waitcnt lgkmcnt(2)\n\t"
      "v_pk_add_f32 v[118:119], v[118:119], v[120:121]\n\t"
      "v_add_f32 v104, v110, v112\n\t"
      "v_pk_add_f32 v[122:123], v[122:123], v[124:125]\n\t"
      "v_mul_f32 v109, 0x2f800000, v104\n\t"
      "s_waitcnt lgkmcnt(0)\n\t"
      "v_pk_add_f32 v[126:127], v[126:127], v[128:129]\n\t"
      "v_pk_add_f32 v[130:131], v[130:131], v[132:133]\n\t"
      "v_pk_add_f32 v[118:119], v[118:119], v[122:123]\n\t"
      "v_rcp_f32 v109, v109\n\t"
      "v_pk_add_f32 v[126:127], v[126:127], v[130:131]\n\t"
      "s_nop 0\n\t"
      "v_add_f32 v100, v118, v126\n\t"
      "v_add_f32 v102, v119, v127\n\t"
      "s_and_b64 vcc, %[m], %[pm]\n\t"
      "s_cbranch_scc0 2f\n\t"
      "global_store_dwordx4 %[l16], v[100:103], %[rec] sc1\n\t"
      "s_mov_b64 exec, 0xff\n\t"
      "global_store_dwordx2 %[d8], v[104:105], %[rec] offset:1024 sc1\n\t"
      "2:\n\t"
      "s_and_b64 vcc, %[m], %[fm]\n\t"
      "s_cbranch_scc0 3f\n\t"
      "v_mul_f32 v110, v100, v109\n\t"
      "v_mul_f32 v111, v102, v109\n\t"
      "v_mul_f32 v110, 0x2f800000, v110\n\t"
      "v_mul_f32 v111, 0x2f800000, v111\n\t"
      "v_cvt_pk_bf16_f32 v110, v110, v111\n\t"
      "global_store_dword %[oo], v110, %[out] sc1\n\t"
      "s_mov_b64 exec, 1\n\t"
      "global_store_dword %[oo], v101, %[ep] sc1\n\t"
      "3:\n\t"
      "s_mov_b64 exec, -1\n\t"
      "s_or_b64 %[fm], %[fm], %[pm]\n\t"
      "s_cbranch_scc1 9f\n\t"
      "s_mov_b64 %[fm], -1\n\t"
      "v_add_u32 %[ra], 0x1000, %[ra]\n\t"
      "v_add_u32 %[da], 32, %[da]\n\t"
      "v_mov_b32 v96, v100\n\t"
      "v_mov_b32 v97, v102\n\t"
      "v_mov_b32 v98, v104\n\t"
      "s_add_u32 %[tk], %[tk], 1\n\t"
      "s_and_b32 %[tk], %[tk], 7\n\t"
      "v_lshl_add_u32 %[wa], %[tk], 9, %[ra]\n\t"
      "v_lshl_add_u32 %[wd], %[tk], 2, %[da]\n\t"
      "s_cbranch_scc0 0b\n\t"
      "s_mul_i32 %[t], %[tk], %[str]\n\t"
      "s_mov_b32 %[cnt], 0x100000\n\t"
      "v_add_u32 v106, %[t], %[l16]\n\t"
      "v_add_u32 v107, %[t], %[d8]\n\t"
      "1:\n\t"
      "global_load_dwordx4 v[100:103], v106, %[rec] sc1\n\t"
      "global_load_dwordx2 v[98:99], v107, %[rec] offset:1024 sc1\n\t"
      "s_waitcnt vmcnt(0)\n\t"
      "v_cmp_ne_u32 vcc, %[tg], v101\n\t"
      "v_cmp_ne_u32 %[m], %[tg], v103\n\t"
      "v_mov_b32 v96, v100\n\t"
      "s_or_b64 vcc, vcc, %[m]\n\t"
      "v_cmp_ne_u32 %[m], %[tg], v99\n\t"
      "v_mov_b32 v97, v102\n\t"
      "s_or_b64 vcc, vcc, %[m]\n\t"
      "s_cbranch_scc0 0b\n\t"
      "s_sub_u32 %[cnt], %[cnt], 1\n\t"
      "s_cbranch_scc0 1b\n\t"
      "s_branch 0b\n\t"
      "9:"
      : [tk] "=&s"(tk), [m] "=&s"(m), [t] "=&s"(t), [cnt] "=&s"(cnt), [fm] "+s"(fm), [ra] "+v"(ra), [da] "+v"(da), [wa] "+v"(wa), [wd] "+v"(wd),
        "+{v[96:97]}"(n2), "+{v98}"(den)
      : [pm] "s"(pm), [one] "v"(c_one), [l16] "v"(c_l16), [d8] "v"(c_d8), [rec] "s"(rec), [oo] "v"(c_oo), [out] "s"(po), [ep] "s"(pe),
        [tg] "s"(tag), [str] "i"(REC_STR)
      : "memory", "vcc", "scc", "v99", "v100", "v101", "v102", "v103", "v104", "v105", "v106", "v107", "v108", "v109", "v110", "v111", "v112",
        "v113", "v114", "v115", "v116", "v117", "v118", "v119", "v120", "v121", "v122", "v123", "v124", "v125", "v126", "v127", "v128",
        "v129", "v130", "v131", "v132", "v133");
  }
}

extern "C" void kernel_launch(void* const* d_in, const int* in_sizes, int n_in, void* d_out, int out_size, void* d_ws,
                              size_t ws_size, hipStream_t stream) {
  rfa_decode<<<H * NSPLIT, NWAVE * 64, 0, stream>>>((const uint16_t*)d_in[0], (const uint16_t*)d_in[1], (const uint16_t*)d_in[2],
                                                    (const uint16_t*)d_in[3], (unsigned char*)d_ws, (uint16_t*)d_out);
}
```
